```python
import torch

def _softmax(x, axis=-1):
    return torch.softmax(x, dim=axis)

def _gelu(x):
    return torch.nn.functional.gelu(x, approximate='tanh')

def _silu(x):
    return torch.nn.functional.silu(x)

def _sigmoid(x):
    return torch.sigmoid(x)

def _layernorm(x, axis=-1, eps=1e-05):
    m = x.mean(dim=axis, keepdim=True)
    v = x.var(dim=axis, keepdim=True, unbiased=False)
    return (x - m) / torch.sqrt(v + eps)

def _scatter_add(tgt, idx, src, axis=0):
    return tgt.clone().index_add_(axis, idx, src)

def _segment_sum(src, seg, n):
    out = torch.zeros((n,) + src.shape[1:], dtype=src.dtype)
    return out.index_add_(0, seg, src)

def _pad(x, pw):
    flat = tuple((v for pair in reversed(pw) for v in pair))
    return torch.nn.functional.pad(x, flat)
kernel_name = 'adaLN_cond__sliding_block_cached__glu_gated_bilinear__decode'

def setup_inputs():

    _w = lambda a: (lambda t: t.to(torch.bfloat16) if t.is_floating_point() else t)(torch.as_tensor(a))

    def _A_setup():
        B, Sq, D = (1, 1, 512)
        Hq, Hkv, Sk, Dh = (32, 8, 32768, 128)
        assert Hq % Hkv == 0, (Hq, Hkv)
        return {'x': _w((lambda *s: torch.randn(*s, dtype=torch.bfloat16) * 0.05)(B, Sq, D)), 'k_cache': _w((lambda *s: torch.randn(*s, dtype=torch.bfloat16) * 0.8)(B, Hkv, Sk, Dh)), 'v_cache': _w((lambda *s: torch.randn(*s, dtype=torch.bfloat16) * 3.2)(B, Hkv, Sk, Dh)), 'wq': _w((lambda *s: torch.randn(*s, dtype=torch.bfloat16) * 0.8)(D, Hq * Dh) * D ** (-0.5)), 'wo': _w((lambda *s: torch.randn(*s, dtype=torch.bfloat16) * 3.2)(Hq * Dh, D) * (Hq * Dh) ** (-0.5))}
    _A_in = _A_setup()

    def _B_setup():
        B, S, D, F = (1, 1, 512, 1792)
        return {'x': _w((lambda *s: torch.randn(*s, dtype=torch.bfloat16) * 0.05)(B, S, D)), 'w_gate': _w((lambda *s: torch.randn(*s, dtype=torch.bfloat16) * 3.2)(D, F) * D ** (-0.5)), 'w_up': _w((lambda *s: torch.randn(*s, dtype=torch.bfloat16) * 3.2)(D, F) * D ** (-0.5)), 'w_down': _w((lambda *s: torch.randn(*s, dtype=torch.bfloat16) * 3.2)(F, D) * F ** (-0.5))}
    _B_in = _B_setup()
    return {'A_x': _A_in['x'], 'A_k_cache': _A_in['k_cache'], 'A_v_cache': _A_in['v_cache'], 'A_wq': _A_in['wq'], 'A_wo': _A_in['wo'], 'B_x': _B_in['x'], 'B_w_gate': _B_in['w_gate'], 'B_w_up': _B_in['w_up'], 'B_w_down': _B_in['w_down'], 'c': _w((lambda *s: torch.randn(*s, dtype=torch.bfloat16) * 0.4)(1, 512) * 8.0), 'w_mod': _w((lambda *s: torch.randn(*s, dtype=torch.bfloat16) * 0.4)(512, 6 * 512) * 512 ** (-0.5) * 8.0)}

def reference(A_x, A_k_cache, A_v_cache, A_wq, A_wo, B_x, B_w_gate, B_w_up, B_w_down, c, w_mod):

    def _A_ref(x, k_cache, v_cache, wq, wo):
        B, Sq, D = x.shape
        Hq, Dh = (32, 128)
        Hkv, Sk = (k_cache.shape[1], k_cache.shape[2])
        W = min(4096, max(256, Sk // 4))
        rep = Hq // Hkv
        q = (lambda x, *d: x.permute(*d))((x @ wq).reshape(B, Sq, Hq, Dh), 0, 2, 1, 3)
        k = (lambda x, n, axis: torch.repeat_interleave(x, n, dim=axis))(k_cache[:, :, Sk - W:, :], rep, axis=1)
        v = (lambda x, n, axis: torch.repeat_interleave(x, n, dim=axis))(v_cache[:, :, Sk - W:, :], rep, axis=1)
        scores = torch.einsum('bhqd,bhkd->bhqk', q, k) * Dh ** (-0.5)
        p = _softmax(scores, axis=-1)
        o = torch.einsum('bhqk,bhkd->bhqd', p, v)
        return (lambda x, *d: x.permute(*d))(o, 0, 2, 1, 3).reshape(B, Sq, Hq * Dh) @ wo

    def _B_ref(x, w_gate, w_up, w_down):
        g = torch.einsum('bsd,df->bsf', x, w_gate)
        u = torch.einsum('bsd,df->bsf', x, w_up)
        h = g * u
        return torch.einsum('bsf,fd->bsd', h, w_down)
    x = A_x
    mod = (_silu(c) @ w_mod).reshape(c.shape[0], 6, 1, -1)
    sa, ca, ga, sb, cb, gb = (mod[:, 0].clone(), mod[:, 1].clone(), mod[:, 2].clone(), mod[:, 3].clone(), mod[:, 4].clone(), mod[:, 5].clone())
    h = _layernorm(x, axis=-1) * (1 + ca) + sa
    x = x + ga * _A_ref(h, A_k_cache, A_v_cache, A_wq, A_wo)
    h = _layernorm(x, axis=-1) * (1 + cb) + sb
    x = x + gb * _B_ref(h, B_w_gate, B_w_up, B_w_down)
    return x
```

```cpp
#include <hip/hip_runtime.h>
#include <hip/hip_bf16.h>
typedef unsigned short u16;
__device__ __forceinline__ float bf2f(u16 v){ return __uint_as_float(((unsigned)v)<<16); }
__device__ __forceinline__ u16 f2bf(float f){ unsigned u=__float_as_uint(f); return (u16)((u+0x7fffu+((u>>16)&1u))>>16); }
constexpr int D=512, HQ=32, HKV=8, SK=32768, DH=128, W=4096, F=1792;

__device__ float block_sum(float v, float* red){
  for(int o=32;o>0;o>>=1) v+=__shfl_xor(v,o);
  __syncthreads();
  if((threadIdx.x&63)==0) red[threadIdx.x>>6]=v;
  __syncthreads();
  float s=0; for(int i=0;i<(int)(blockDim.x>>6);++i) s+=red[i];
  return s;
}
__device__ void gemv64(const float* xs, const u16* w, int K, int N, int n0, float* red, float* outv){
  int lane=threadIdx.x&63, g=threadIdx.x>>6;
  float acc=0;
  for(int k=g;k<K;k+=4) acc+=xs[k]*bf2f(w[(size_t)k*N+n0+lane]);
  red[g*64+lane]=acc; __syncthreads();
  if(g==0) *outv=red[lane]+red[64+lane]+red[128+lane]+red[192+lane];
  __syncthreads();
}
__global__ void k_mod(const u16* c,const u16* wmod,float* mod){
  __shared__ float xs[512]; __shared__ float red[256];
  for(int i=threadIdx.x;i<512;i+=256){ float v=bf2f(c[i]); xs[i]=v/(1.f+__expf(-v)); }
  __syncthreads();
  float o; gemv64(xs,wmod,512,3072,blockIdx.x*64,red,&o);
  if(threadIdx.x<64) mod[blockIdx.x*64+threadIdx.x]=o;
}
__device__ void ln_mod(const float* xin, const float* shift, const float* scale, float* xs, float* red){
  float a=xin[threadIdx.x], b=xin[threadIdx.x+256];
  float m=block_sum(a+b,red)*(1.f/512);
  float v=block_sum((a-m)*(a-m)+(b-m)*(b-m),red)*(1.f/512);
  float r=1.f/sqrtf(v+1e-5f);
  xs[threadIdx.x]=(a-m)*r*(1.f+scale[threadIdx.x])+shift[threadIdx.x];
  xs[threadIdx.x+256]=(b-m)*r*(1.f+scale[threadIdx.x+256])+shift[threadIdx.x+256];
  __syncthreads();
}
__global__ void k_q(const u16* x,const float* mod,const u16* wq,float* q){
  __shared__ float xs[512]; __shared__ float xi[512]; __shared__ float red[256];
  for(int i=threadIdx.x;i<512;i+=256) xi[i]=bf2f(x[i]);
  __syncthreads();
  ln_mod(xi,mod,mod+512,xs,red);
  float o; gemv64(xs,wq,512,4096,blockIdx.x*64,red,&o);
  if(threadIdx.x<64) q[blockIdx.x*64+threadIdx.x]=o;
}
__global__ void k_attn(const float* q,const u16* kc,const u16* vc,float* po,float* pm,float* pl){
  int h=blockIdx.x>>5, s=blockIdx.x&31, kv=h>>2;
  __shared__ float qs[128]; __shared__ float ps[128]; __shared__ float red[4];
  int t=threadIdx.x; qs[t]=q[h*128+t]; __syncthreads();
  const u16* kp=kc+((size_t)kv*SK+(SK-W)+s*128+t)*DH;
  float sc=0; for(int d=0;d<128;++d) sc+=qs[d]*bf2f(kp[d]);
  sc*=0.08838834764831845f;
  float m=sc; for(int o=32;o>0;o>>=1) m=fmaxf(m,__shfl_xor(m,o));
  if((t&63)==0) red[t>>6]=m; __syncthreads(); m=fmaxf(red[0],red[1]);
  float p=__expf(sc-m); ps[t]=p;
  float l=block_sum(p,red+2);
  const u16* vp=vc+((size_t)kv*SK+(SK-W)+s*128)*DH+t;
  float acc=0; for(int k=0;k<128;++k) acc+=ps[k]*bf2f(vp[(size_t)k*DH]);
  po[(h*32+s)*128+t]=acc; if(t==0){pm[h*32+s]=m; pl[h*32+s]=l;}
}
__global__ void k_comb(const float* po,const float* pm,const float* pl,float* o){
  int h=blockIdx.x,t=threadIdx.x; float M=-1e30f;
  for(int s=0;s<32;++s) M=fmaxf(M,pm[h*32+s]);
  float L=0,acc=0; for(int s=0;s<32;++s){ float w=__expf(pm[h*32+s]-M); L+=w*pl[h*32+s]; acc+=w*po[(h*32+s)*128+t]; }
  o[h*128+t]=acc/L;
}
__global__ void k_wo(const float* o,const u16* wo,const u16* x,const float* mod,float* x1){
  __shared__ float xs[4096]; __shared__ float red[256];
  for(int i=threadIdx.x;i<4096;i+=256) xs[i]=o[i];
  __syncthreads();
  float r; gemv64(xs,wo,4096,512,blockIdx.x*64,red,&r);
  if(threadIdx.x<64){int n=blockIdx.x*64+threadIdx.x; x1[n]=bf2f(x[n])+mod[1024+n]*r;}
}
__global__ void k_gu(const float* x1,const float* mod,const u16* wg,const u16* wu,float* hh){
  __shared__ float xs[512]; __shared__ float xi[512]; __shared__ float red[256];
  for(int i=threadIdx.x;i<512;i+=256) xi[i]=x1[i];
  __syncthreads();
  ln_mod(xi,mod+3*512,mod+4*512,xs,red);
  float g,u; gemv64(xs,wg,512,F,blockIdx.x*64,red,&g); gemv64(xs,wu,512,F,blockIdx.x*64,red,&u);
  if(threadIdx.x<64) hh[blockIdx.x*64+threadIdx.x]=g*u;
}
__global__ void k_down(const float* hh,const u16* wd,const float* x1,const float* mod,u16* out){
  __shared__ float xs[F]; __shared__ float red[256];
  for(int i=threadIdx.x;i<F;i+=256) xs[i]=hh[i];
  __syncthreads();
  float r; gemv64(xs,wd,F,512,blockIdx.x*64,red,&r);
  if(threadIdx.x<64){int n=blockIdx.x*64+threadIdx.x; out[n]=f2bf(x1[n]+mod[5*512+n]*r);}
}
extern "C" void kernel_launch(void* const* d_in, const int* in_sizes, int n_in, void* d_out, int out_size, void* d_ws, size_t ws_size, hipStream_t stream){
  const u16* x=(const u16*)d_in[0]; const u16* kc=(const u16*)d_in[1]; const u16* vc=(const u16*)d_in[2];
  const u16* wq=(const u16*)d_in[3]; const u16* wo=(const u16*)d_in[4];
  const u16* wg=(const u16*)d_in[6]; const u16* wu=(const u16*)d_in[7]; const u16* wd=(const u16*)d_in[8];
  const u16* c=(const u16*)d_in[9]; const u16* wmod=(const u16*)d_in[10];
  float* ws=(float*)d_ws; float* mod=ws; float* q=ws+4096; float* o=ws+8192; float* x1=ws+12288; float* hh=ws+16384; float* pm=ws+20480; float* pl=ws+24576; float* po=ws+32768;
  k_mod<<<48,256,0,stream>>>(c,wmod,mod);
  k_q<<<64,256,0,stream>>>(x,mod,wq,q);
  k_attn<<<1024,128,0,stream>>>(q,kc,vc,po,pm,pl);
  k_comb<<<32,128,0,stream>>>(po,pm,pl,o);
  k_wo<<<8,256,0,stream>>>(o,wo,x,mod,x1);
  k_gu<<<28,256,0,stream>>>(x1,mod,wg,wu,hh);
  k_down<<<8,256,0,stream>>>(hh,wd,x1,mod,(u16*)d_out);
}
```

```cpp
#include <hip/hip_runtime.h>
typedef unsigned u32; typedef unsigned short u16; typedef unsigned long long u64;
typedef u32 u32x2 __attribute__((ext_vector_type(2)));
typedef u32 u32x4 __attribute__((ext_vector_type(4)));
typedef u32 u32x8 __attribute__((ext_vector_type(8)));
typedef float f32x4 __attribute__((ext_vector_type(4)));
typedef float f32x2 __attribute__((ext_vector_type(2)));
typedef __bf16 bf16x2 __attribute__((ext_vector_type(2)));
typedef __bf16 bf16x8 __attribute__((ext_vector_type(8)));
typedef __amdgpu_buffer_rsrc_t rsrc_t;
#define DI __device__ __forceinline__
constexpr int AUXW = 2;
constexpr u32 T_K = 70, T_Q = 380, T_M = 600;
constexpr u32 CODE_LEN = 15488;
constexpr u32 A_STR = 294912, R_STR = 16384;
constexpr int SC1 = 16;
constexpr u32 TAGM = 0x3FFu;
constexpr int SPIN_MAX = 20000;
constexpr u32 WS_EPOCH = 0;
constexpr u32 WS_A = 4096;
constexpr u32 WS_Q = WS_A + 8 * A_STR;
constexpr u32 WS_O = WS_Q + 32768;
constexpr u32 O_STR = 2176;
constexpr u32 WS_M = WS_O + 128 * O_STR;
constexpr u32 WS_D = WS_M + 8 * R_STR;
constexpr u32 WS_E = WS_D + 8 * R_STR;
constexpr u32 WS_END = WS_E + 56 * 2048;

DI rsrc_t mk(const void* p, u32 n) { return __builtin_amdgcn_make_buffer_rsrc((void*)p, 0, n, 0x00020000); }
DI float asf(u32 u) { return __builtin_bit_cast(float, u); }
DI u32 asu(float f) { return __builtin_bit_cast(u32, f); }
DI u32 pk(float a, float b) { f32x2 v = {a, b}; return __builtin_bit_cast(u32, __builtin_convertvector(v, bf16x2)); }
DI float rb(float a) { return asf(pk(a, a) << 16); }
DI float blo(u32 u) { return asf(u << 16); }
DI float bhi(u32 u) { return asf(u & 0xFFFF0000u); }
DI u32 tagf(float f, u32 T) { return ((asu(f) + 0x200u) & ~TAGM) | T; }
DI float fmx(float a, float b) { return __builtin_amdgcn_fmed3f(a, b, __builtin_inff()); }
DI void sched() { __builtin_amdgcn_sched_barrier(0); }
DI void fence() { asm volatile("" ::: "memory"); }
DI void bar0() { asm volatile("s_barrier" ::: "memory"); }
DI void bar() { asm volatile("s_waitcnt lgkmcnt(0)\n\ts_barrier" ::: "memory"); }
template <int C> DI float dpp(float v) { return asf(__builtin_amdgcn_update_dpp(0, asu(v), C, 0xF, 0xF, true)); }
constexpr int ROR1 = 0x121, ROR2 = 0x122, ROR4 = 0x124, ROR8 = 0x128;
DI float row_sum(float v) { v += dpp<ROR8>(v); v += dpp<ROR4>(v); v += dpp<ROR2>(v); v += dpp<ROR1>(v); return v; }
DI float row_max(float v) { v = fmx(v, dpp<ROR8>(v)); v = fmx(v, dpp<ROR4>(v)); v = fmx(v, dpp<ROR2>(v)); v = fmx(v, dpp<ROR1>(v)); return v; }
DI f32x2 sw32(float a, float b) { auto r = __builtin_amdgcn_permlane32_swap(asu(a), asu(b), false, false); return f32x2{asf(r[0]), asf(r[1])}; }
DI f32x2 sw16(float a, float b) { auto r = __builtin_amdgcn_permlane16_swap(asu(a), asu(b), false, false); return f32x2{asf(r[0]), asf(r[1])}; }
DI float col_sum(float v) { f32x2 r = sw32(v, v); v = r[0] + r[1]; r = sw16(v, v); return r[0] + r[1]; }
DI float col_max(float v) { f32x2 r = sw32(v, v); v = fmx(r[0], r[1]); r = sw16(v, v); return fmx(r[0], r[1]); }
DI float rs4(f32x4 v) { f32x2 a = sw32(v[0], v[2]), b = sw32(v[1], v[3]); f32x2 c = sw16(a[0] + a[1], b[0] + b[1]); return c[0] + c[1]; }
DI f32x4 asf4(u32x4 v) { return __builtin_bit_cast(f32x4, v); }
DI f32x4 mfma(u32x4 a, u32x4 b, f32x4 c) {
  return __builtin_amdgcn_mfma_f32_16x16x32_bf16(__builtin_bit_cast(bf16x8, a), __builtin_bit_cast(bf16x8, b), c, 0, 0, 0);
}
DI void tr8(const u32x4 (&W)[8], u32x4 (&O)[8]) {
#pragma unroll
  for (int jj = 0; jj < 4; ++jj)
#pragma unroll
    for (int c = 0; c < 8; ++c) O[c][jj] = __builtin_amdgcn_perm(W[2 * jj + 1][c >> 1], W[2 * jj][c >> 1], (c & 1) ? 0x07060302u : 0x05040100u);
}
template <int N> DI void pin(u32x4 (&v)[N]) {
#pragma unroll
  for (int i = 0; i < N; ++i) asm volatile("" : "+v"(v[i]));
}
DI f32x2 rb2(f32x2 v) { u32 p = pk(v[0], v[1]); return f32x2{blo(p), bhi(p)}; }
DI float opq(float v) { asm("" : "+v"(v)); return v; }
template <int N> DI void vmwait() { asm volatile("s_waitcnt vmcnt(%0)" ::"n"(N) : "memory"); }
DI void wait_until(u32 t0, u32 ticks) {
  while ((u32)__builtin_amdgcn_s_memrealtime() - t0 < ticks) __builtin_amdgcn_s_sleep(1);
  fence();
}
template <int N> DI void gather(u32x4 (&v)[N], rsrc_t ws, u32 off, const u32 (&so)[N], u32 T) {
  int spins = 0;
  while (true) {
    fence();
    u32 bad = 0;
#pragma unroll
    for (int i = 0; i < N; ++i) v[i] = __builtin_amdgcn_raw_buffer_load_b128(ws, off, so[i], SC1);
#pragma unroll
    for (int i = 0; i < N; ++i) { v[i][1] ^= T; v[i][3] ^= T; bad |= v[i][1] | v[i][3]; }
    if (__builtin_amdgcn_ballot_w64((bad & TAGM) != 0) == 0 || ++spins > SPIN_MAX) break;
  }
}
template <int N, int SL> DI void gather2(u32x4 (&v)[N], rsrc_t ws, u32 off, const u32 (&so)[N], u32 T) {
  u32x4 a[N], b[N];
#define G_LD(r) fence(); _Pragma("unroll") for (int i = 0; i < N; ++i) r[i] = __builtin_amdgcn_raw_buffer_load_b128(ws, off, so[i], SC1); fence()
#define G_OK(r) ({ u32 bad = 0; _Pragma("unroll") for (int i = 0; i < N; ++i) { v[i] = r[i]; v[i][1] ^= T; v[i][3] ^= T; bad |= v[i][1] | v[i][3]; } __builtin_amdgcn_ballot_w64((bad & TAGM) != 0) == 0; })
  G_LD(a);
  __builtin_amdgcn_s_sleep(SL);
  for (int spins = 0;; ++spins) {
    G_LD(b);
    if (G_OK(a)) break;
    G_LD(a);
    if (G_OK(b) || spins > SPIN_MAX) break;
  }
}
template <int NT> struct Tile { u32x4 W[NT][8]; u32x4 O[NT][8]; };
template <int NT> DI void tile_load(Tile<NT>& m, rsrc_t r, u32 base, u32 stride, int lane) {
  const u32 off = base + (lane >> 3) * 8 * stride + (lane & 7) * 16;
#pragma unroll
  for (int t = 0; t < NT; ++t)
#pragma unroll
    for (int j = 0; j < 8; ++j) m.W[t][j] = __builtin_amdgcn_raw_buffer_load_b128(r, off, (t * 64 + j) * stride, AUXW);
}
template <int NT> DI void tile_tr(Tile<NT>& m) {
#pragma unroll
  for (int t = 0; t < NT; ++t) { tr8(m.W[t], m.O[t]); pin(m.O[t]); }
}
DI int quad(int n) { return (n & 7) * 2 + (n >> 3); }
template <int NT> DI f32x4 tile_mul(const Tile<NT>& m, const char* xs, int lane) {
  f32x4 a[8];
#pragma unroll
  for (int t = 0; t < NT; ++t) {
    const u32x4 X = *(const u32x4*)(xs + t * 128 + (lane >> 4) * 32 + (lane & 1) * 16);
#pragma unroll
    for (int c = 0; c < 8; ++c) a[(c >> 1) + (c & 1) * 4] = mfma(X, m.O[t][(c >> 1) + (c & 1) * 4], t ? a[(c >> 1) + (c & 1) * 4] : f32x4{0, 0, 0, 0});
  }
  f32x4 r;
#pragma unroll
  for (int c = 0; c < 4; ++c) r[c] = dpp<ROR8>((lane & 8) ? a[c][1] : a[c + 4][0]) + ((lane & 8) ? a[c + 4][1] : a[c][0]);
  return r;
}
DI u32x4 tag4(f32x4 a, u32 T) { return u32x4{asu(a[0]), tagf(a[1], T), asu(a[2]), tagf(a[3], T)}; }
DI u32x4 join64(float* part, f32x4 a, u32 T, int w, int lane) {
  f32x4* p = (f32x4*)part + (lane & 15);
  p[w * 16] = a;
  bar();
  return tag4((p[0] + p[16]) + (p[32] + p[48]), T);
}
DI u32 touch(rsrc_t r, u32 off) { return __builtin_amdgcn_raw_buffer_load_b32(r, off, 0, 0); }
DI void keep(u32 v) { asm volatile("" ::"v"(v)); }
DI float silu(float v) { return v * __builtin_amdgcn_rcpf(1.f + __expf(-v)); }
DI float inv_sd(float var) { return __builtin_amdgcn_rcpf(rb(__builtin_amdgcn_sqrtf(rb(rb(var) + 1e-5f)))); }
DI u32 blk(u32 j) { return j * 1024; }
constexpr float LOG2E = 1.4426950408889634f;

__global__ __launch_bounds__(320, 2) void fused(const char* px, const char* pk_, const char* pv, const char* pq, const char* pc, const char* pm, char* pws,
                                             const char* po, const char* pg, const char* pu, const char* pd, char* pout) {
  __shared__ __attribute__((aligned(16))) char lds[84 * 1024];
  const int wg = blockIdx.x, lane = threadIdx.x & 63, w = __builtin_amdgcn_readfirstlane(threadIdx.x >> 6);
  const bool att = wg < 128, wk = w < 4;
  u64 a0, a1, a2, tm;
  u32 a3;
  asm volatile("s_load_dwordx2 %0, %5, %6\n\ts_load_dwordx2 %1, %5, %7\n\ts_load_dwordx2 %2, %5, 0x30\n\ts_load_dword %3, %5, 0x40\n\ts_memrealtime %4"
               : "=&s"(a0), "=&s"(a1), "=&s"(a2), "=&s"(a3), "=&s"(tm)
               : "s"(__builtin_amdgcn_kernarg_segment_ptr()), "s"(att ? 8 : 24 + (w & 4) * 4), "s"((w & 4) * 8));
  const int x = wg & 7, i = wg >> 3, g = lane >> 4, n = lane & 15;
  const int k = i & 7, hb = (i >> 3) & 1;
  u32 p0 = 0;
  if (att && !wk) {
    u64 pc0;
    asm volatile("s_getpc_b64 %0" : "=s"(pc0));
    p0 = touch(mk((const void*)(pc0 & ~127ull), CODE_LEN), lane * 128 + (i & 1) * 8192);
  }
  u32 kvb = (u32)(x * 32768 + 28672) * 256u + (i * 4 + w) * 16384, bb = (hb * 4 + w) * 524288 + (x * 8 + k) * 128, ab = k * 64 * 6144 + (x * 2 + hb) * 128;
  u32 rot = blk((2 * x + (i & 1) + 4 * w + 8 * (i >> 1)) & 15), s0 = 8192 - (w & 4) * 512;
  u32 o0 = (n >> 2) * 256 + (g * 4 + (n & 3)) * 16, o1 = (lane >> 3) * 8 * s0 + (lane & 7) * 16, o2 = lane * 16, o3 = k * 128 + (lane & 31) * 4;
  asm volatile("" : "+s"(kvb), "+s"(bb), "+s"(ab), "+s"(rot), "+s"(s0));
  asm volatile("" : "+v"(o0), "+v"(o1), "+v"(o2), "+v"(o3));
  o0 = (att ? o0 : o1) + (att ? kvb : wk ? bb : ab);
  o2 = wk ? o2 : o3;
  rot = att ? rot : 0;
  s0 = att ? 1024 : s0;
  u32 so[8];
#pragma unroll
  for (int j = 0; j < 8; ++j) so[j] = (j * s0) ^ rot;
  asm volatile("" : "+v"(o0), "+v"(o2));
  asm volatile("s_waitcnt lgkmcnt(0)" : "+s"(a0), "+s"(a1), "+s"(a2), "+s"(a3), "+s"(tm), "+s"(so[0]), "+s"(so[1]), "+s"(so[2]),
               "+s"(so[3]), "+s"(so[4]), "+s"(so[5]), "+s"(so[6]), "+s"(so[7]));
  const u32 t0 = (u32)tm;
  const rsrc_t rws = mk((const void*)a2, WS_END), r0 = mk((const void*)a0, att && !wk ? 0 : 64u << 20);
  if (!att && wk) bar0();
  if (att) wait_until(t0, T_K);
  const u32x4 xa = __builtin_amdgcn_raw_buffer_load_b128(mk((const void*)a1, att ? 0 : 1024), o2, 0, 0);
  const u32 ev = __builtin_amdgcn_raw_buffer_load_b32(rws, WS_EPOCH, 0, 0);
  fence();
  Tile<1> B;
#pragma unroll
  for (int j = 0; j < 8; ++j) B.W[0][j] = __builtin_amdgcn_raw_buffer_load_b128(r0, o0, so[j], AUXW);
  fence();
  if (!att && !wk) bar0();
#define TAG() ((__builtin_amdgcn_readfirstlane(ev) + 1u) & TAGM)
  float* part = (float*)lds;
  float* part2 = (float*)(lds + 8192);
  char* xs = lds + 10240 + w * 512;
  float* sm = (float*)(lds + 13312);
  char* qs = lds + 14336;
  float* ms = (float*)(lds + 16384);

  if (att) {
    const int id = x * 16 + i;
    if (w < 4) {
      u32x4 K[16], V[2][8];
      const u32 vo = kvb + lane * 16;
      const rsrc_t rv = mk(pv, 64u << 20);
#pragma unroll
      for (int j = 0; j < 8; ++j) { K[j] = B.W[0][j]; K[j + 8] = __builtin_amdgcn_raw_buffer_load_b128(r0, o0, blk(j + 8) ^ rot, AUXW); }
      fence();
#define VLOAD(t) _Pragma("unroll") for (int j = 0; j < 8; ++j) V[t][j] = __builtin_amdgcn_raw_buffer_load_b128(rv, vo, blk(t * 8 + (j >> 1) + (j & 1) * 4) ^ rot, AUXW)
      VLOAD(0);
      VLOAD(1);
      const u32 T = TAG();
      asm volatile("" ::"s"(T));
      u32x4 Vt[2][8];
      u32 oo = lane < 32 ? WS_O + id * O_STR + w * 512 + lane * 16 : WS_END, pa = w * 512 + (lane & 31) * 16;
      asm volatile("" : "+v"(oo), "+v"(pa));
      pin(K);
      tr8(V[0], Vt[0]);
      pin(Vt[0]);
      bar();
      const u32x4 Q = *(const u32x4*)(qs + (n >> 2) * 256 + (g * 4 + (n & 3)) * 16);
      float s[4];
      f32x4 a[16];
#pragma unroll
      for (int r = 0; r < 5; ++r) {
        float t[4];
#define QK(u, c0, c1, c2, c3)                                                                          \
  if (r < 4) a[4 * r + u] = mfma(K[4 * r + u], Q, f32x4{0, 0, 0, 0});                                   \
  if (r) { const f32x4 b = a[4 * r - 4 + u]; t[u] = opq(dpp<c3 * 0x55>(b[c3]) + opq(dpp<c2 * 0x55>(b[c2]) + opq(dpp<c1 * 0x55>(b[c1]) + b[c0]))); } \
  sched()
        QK(0, 0, 1, 2, 3); QK(1, 1, 0, 2, 3); QK(2, 2, 0, 1, 3); QK(3, 3, 0, 1, 2);
        if (r) s[r - 1] = (n & 2) ? ((n & 1) ? t[3] : t[2]) : ((n & 1) ? t[1] : t[0]);
      }
      f32x2 s0 = rb2(rb2(f32x2{s[0], s[1]}) * 0.08838834764831845f), s1 = rb2(rb2(f32x2{s[2], s[3]}) * 0.08838834764831845f);
      float mx = fmx(fmx(s0[0], s0[1]), fmx(s1[0], s1[1]));
      mx = fmx(mx, dpp<0xB1>(mx));
      mx = col_max(fmx(mx, dpp<0x4E>(mx)));
      s0 = (s0 - mx) * LOG2E; s1 = (s1 - mx) * LOG2E;
      s0 = f32x2{__builtin_amdgcn_exp2f(s0[0]), __builtin_amdgcn_exp2f(s0[1])};
      s1 = f32x2{__builtin_amdgcn_exp2f(s1[0]), __builtin_amdgcn_exp2f(s1[1])};
      float l = (s0[0] + s1[0]) + (s0[1] + s1[1]);
      l += dpp<0xB1>(l);
      l = col_sum(l + dpp<0x4E>(l));
      *(f32x2*)(sm + ((n >> 2) * 4 + w) * 2) = f32x2{mx, l};
      bar();
      sched();
      f32x4 o[8];
#pragma unroll
      for (int t = 0; t < 2; ++t) {
        const float p = asf(t ? pk(s1[0], s1[1]) : pk(s0[0], s0[1]));
        const u32x4 P = {asu(p), asu(dpp<0x55>(p)), asu(dpp<0xAA>(p)), asu(dpp<0xFF>(p))};
        if (t) tr8(V[t], Vt[t]);
#pragma unroll
        for (int c = 0; c < 8; ++c) o[c] = mfma(P, Vt[t][c], t ? o[c] : f32x4{0, 0, 0, 0});
      }
      *(f32x4*)(part + w * 512 + lane * 8) = f32x4{o[0][0], o[1][0], o[2][0], o[3][0]};
      *(f32x4*)(part + w * 512 + lane * 8 + 4) = f32x4{o[4][0], o[5][0], o[6][0], o[7][0]};
      bar();
      {
        const f32x4* p = (const f32x4*)(lds + pa);
        const f32x4 f = *(const f32x4*)(sm + 32 + w * 4);
        const f32x4 r = p[0] * f[0] + p[128] * f[1] + p[256] * f[2] + p[384] * f[3];
        __builtin_amdgcn_raw_buffer_store_b128(tag4(r, T), rws, oo, 0, SC1);
      }
      __builtin_amdgcn_s_sleep(2);
      fence();
      keep(touch(mk(i >> 3 ? pu : pg, 512 * 3584), ((i & 7) * 64 + lane) * 3584 + (w < 3 ? x * 3 + w : x < 4 ? 24 + x : 16384) * 128));
    } else {
      __builtin_amdgcn_s_setprio(3);
      const u32 T = TAG();
      keep(p0);
      {
        u32x4 v[4];
        wait_until(t0, T_Q);
        gather<4>(v, rws, WS_Q + x * 4096 + lane * 16, {0, 1024, 2048, 3072}, T);
#pragma unroll
        for (int q = 0; q < 2; ++q) {
          const f32x4 a = asf4(v[q]) + asf4(v[q + 2]);
          *(u32x2*)(qs + q * 512 + lane * 8) = u32x2{pk(a[0], a[1]), pk(a[2], a[3])};
        }
      }
      bar();
      bar();
      {
        const f32x2 a = *(const f32x2*)(sm + n * 2);
        float mx = fmx(a[0], dpp<0xB1>(a[0]));
        mx = fmx(mx, dpp<0x4E>(mx));
        const float f = __expf(a[0] - mx);
        sm[32 + n] = f;
        float l = f * a[1];
        l += dpp<0xB1>(l);
        l += dpp<0x4E>(l);
        if (lane < 16) __builtin_amdgcn_raw_buffer_store_b64(u32x2{asu(l), asu(mx) | T}, rws, WS_O + id * O_STR + 2048 + lane * 8, 0, SC1);
      }
      bar();
    }
  } else {
    const int id = x * 8 + k;
    if (w < 4) {
      const rsrc_t rx = mk(px, 1024), rc = mk(pc, 1024);
      const u32 T = TAG();
      asm volatile("" ::"s"(T));
      *(u32x4*)(lds + 24576 + w * 1024 + lane * 16) = xa;
      fence();
      const u32 xe = *(const u16*)(lds + 24576 + w * 1024 + (hb * 256 + w * 64 + n * 4 + g) * 2);
      float mean = 0, var = 0;
#pragma unroll
      for (int q = 0; q < 4; ++q) mean += blo(xa[q]) + bhi(xa[q]);
      mean = col_sum(row_sum(mean)) * (1.f / 512);
#pragma unroll
      for (int q = 0; q < 4; ++q) { float a = blo(xa[q]) - mean, b = bhi(xa[q]) - mean; var += a * a + b * b; }
      var = col_sum(row_sum(var)) * (1.f / 512);
      float y = rb(rb(blo(xe) - rb(mean)) * inv_sd(var));
      asm volatile("" : "+v"(y));
      tile_tr(B);
      {
        u32x4 v[4];
        gather<4>(v, rws, WS_A + x * A_STR + g * 4096 + hb * 1024 + w * 256 + n * 16, {0, 2048, 16384, 18432}, T);
        const f32x2 m = rb2(f32x2{rs4(asf4(v[0]) + asf4(v[2])), rs4(asf4(v[1]) + asf4(v[3]))});
        const float h = rb(y * rb(1.f + m[1])) + m[0];
        *(u16*)(xs + (n * 4 + g) * 2) = (u16)pk(h, h);
        fence();
        const u32x4 r = join64(part2, tile_mul(B, xs, lane), T, w, lane);
        if (threadIdx.x < 16) __builtin_amdgcn_raw_buffer_store_b128(r, rws, WS_Q + x * 4096 + hb * 2048 + k * 256 + quad(n) * 16, 0, SC1);
      }
      if (w) __builtin_amdgcn_s_sleep(2);
      if (!hb) {
        fence();
        Tile<2> D;
        tile_load(D, mk(po, 4096 * 1024), (x * 512 + w * 128) * 1024 + k * 128, 1024, lane);
        tile_tr(D);
        u32x4 u[1], v[8];
        gather<1>(u, rws, WS_O + (x * 16 + g + (n & 3) * 4) * O_STR + 2048 + w * 32 + (n & 4) * 4, {0}, T);
        const float m = asf(u[0][1]);
        float mx = fmx(m, dpp<0xB1>(m));
        mx = col_max(fmx(mx, dpp<0x4E>(mx)));
        float f = __expf(m - mx), l = f * asf(u[0][0]);
        l += dpp<0xB1>(l);
        f *= __builtin_amdgcn_rcpf(col_sum(l + dpp<0x4E>(l)));
        float f0 = dpp<0x00>(f), f1 = dpp<0x55>(f), f2 = dpp<0xAA>(f), f3 = dpp<0xFF>(f);
        asm volatile("" : "+v"(f0), "+v"(f1), "+v"(f2), "+v"(f3));
        gather<8>(v, rws, WS_O + (x * 16 + g) * O_STR + w * 512 + n * 16,
                  {0, 4 * O_STR, 8 * O_STR, 12 * O_STR, 256, 4 * O_STR + 256, 8 * O_STR + 256, 12 * O_STR + 256}, T);
        const u32 ov = pk(rs4(asf4(v[0]) * f0 + asf4(v[1]) * f1 + asf4(v[2]) * f2 + asf4(v[3]) * f3),
                          rs4(asf4(v[4]) * f0 + asf4(v[5]) * f1 + asf4(v[6]) * f2 + asf4(v[7]) * f3));
        *(u16*)(xs + (n * 4 + g) * 2) = (u16)ov;
        *(u16*)(xs + 128 + (n * 4 + g) * 2) = (u16)(ov >> 16);
        fence();
        const u32x4 r = join64(part, tile_mul(D, xs, lane), T, w, lane);
        if (lane < 32) __builtin_amdgcn_raw_buffer_store_b128(r, rws, WS_D + (w * 2 + g) * R_STR + x * 2048 + k * 256 + quad(n) * 16, 0, SC1);
      } else {
        fence();
        const u32 c2 = __builtin_amdgcn_raw_buffer_load_b32(rc, (id & 1) * 512 + w * 128 + (lane & 31) * 4, 0, 0);
        const u32 x2 = *(const u32*)(lds + 24576 + w * 1280 + (lane & 31) * 8 + (lane >> 5) * 4);
        fence();
        tile_load(B, mk(pm, 512 * 6144), ((id & 1) * 256 + w * 64) * 6144 + 2048 + (id >> 1) * 128, 6144, lane);
        fence();
        const int e = (k < 6 ? x * 6 : 42 + x * 2) + k;
        u32x4 Dw[8], Dt[8];
        const rsrc_t rd = mk(pd, 1792 * 1024);
#define DLOAD() _Pragma("unroll") for (int j = 0; j < 8; ++j) Dw[j] = __builtin_amdgcn_raw_buffer_load_b128(rd, (e * 32 + g * 8) * 1024 + w * 256 + n * 16, j * 1024, AUXW)
        *(u32*)(xs + 128 + (lane & 31) * 4) = pk(silu(blo(c2)), silu(bhi(c2)));
        tile_tr(B);
        const u32x4 r = join64(part, tile_mul(B, xs + 128, lane), T, w, lane);
        __builtin_amdgcn_raw_buffer_store_b128(r, rws, lane < 32 ? WS_M + (w * 2 + g) * R_STR + (id & 1) * 8192 + (id >> 1) * 256 + quad(n) * 16 : WS_END, 0, SC1);
        if (k < 6 || x < 4) {
        __builtin_amdgcn_s_sleep(2);
        fence();
        Tile<2> G;
        DLOAD();
        fence();
        typedef const __attribute__((address_space(1))) u32x4* gptr;
        const u64 p = (u64)((lane & 4) ? pu : pg) + (w * 128 + (lane >> 3) * 8) * 3584 + e * 64 + (lane & 3) * 16;
#pragma unroll
        for (int t = 0; t < 2; ++t)
#pragma unroll
          for (int j = 0; j < 8; ++j) G.W[t][j] = __builtin_nontemporal_load((gptr)(p + (t * 64 + j) * 3584));
        fence();
        tr8(Dw, Dt);
        pin(Dt);
        tile_tr(G);
        bar();
        const int r0 = (lane & 31) * 4 + (lane >> 5) * 2;
        f32x2 ga = *(const f32x2*)(ms + w * 128 + r0), sb = *(const f32x2*)(ms + 512 + w * 128 + r0), cb = *(const f32x2*)(ms + 1024 + w * 128 + r0);
        asm volatile("" : "+v"(ga), "+v"(sb), "+v"(cb));
        u32x4 v[4];
        gather<4>(v, rws, WS_D + x * R_STR + (lane >> 5) * 2048 + w * 512 + (lane & 31) * 16, {0, 4096, 2 * 4096, 3 * 4096}, T);
        const f32x4 a = (asf4(v[0]) + asf4(v[1])) + (asf4(v[2]) + asf4(v[3]));
        const f32x2 u0 = sw32(a[0], a[2]), u1 = sw32(a[1], a[3]);
        const f32x2 x1 = rb2(f32x2{blo(x2), bhi(x2)} + rb2(ga * rb2(f32x2{u0[0] + u0[1], u1[0] + u1[1]})));
        {
          f32x2 q = sw32(x1[0] + x1[1], x1[0] * x1[0] + x1[1] * x1[1]);
          const float t = q[0] + q[1];
          q = sw16(t, t);
          sm[w * 2 + (lane >> 5)] = row_sum(q[0] + q[1]);
        }
        bar();
        const f32x4 t4 = *(const f32x4*)sm + *(const f32x4*)(sm + 4);
        const float mu = (t4[0] + t4[2]) * (1.f / 512), rs = inv_sd(fmx((t4[1] + t4[3]) * (1.f / 512) - mu * mu, 0.f));
        const f32x2 h = rb2(rb2(rb2(x1 - rb(mu)) * rs) * cb) + sb;
        *(u32*)(xs + r0 * 2) = pk(h[0], h[1]);
        fence();
        *(f32x4*)(part + w * 64 + quad(n) * 4) = tile_mul(G, xs, lane);
        bar();
        const float gu = rb(rs4(*(const f32x4*)(part + lane * 4))), hh = gu * dpp<ROR8>(gu);
        *(u16*)(xs + 256 + ((n & 7) * 4 + g) * 2) = (u16)pk(hh, hh);
        fence();
        const u32x4 X = *(const u32x4*)(xs + 256 + g * 16);
        float d[8];
#pragma unroll
        for (int c = 0; c < 8; ++c) d[c] = mfma(X, Dt[c], f32x4{0, 0, 0, 0})[0];
        const bool up = g & 1;
        const u32x4 o = {asu(up ? d[4] : d[0]), tagf(up ? d[5] : d[1], T), asu(up ? d[6] : d[2]), tagf(up ? d[7] : d[3], T)};
        if (lane < 32) __builtin_amdgcn_raw_buffer_store_b128(o, rws, WS_E + e * 2048 + w * 512 + n * 32 + g * 16, 0, SC1);
        }
      }
    } else {
      __builtin_amdgcn_s_setprio(3);
      const u32 c2 = xa[0];
      *(u32*)(xs + (lane & 31) * 4) = pk(silu(blo(c2)), silu(bhi(c2)));
      fence();
      tr8(B.W[0], B.O[0]);
      const f32x4 a = tile_mul(B, xs, lane);
      const u32 T = TAG();
      const u32x4 o = tag4(a, T);
      const u32 off = WS_A + g * A_STR + k * 4096 + (x * 2 + hb) * 256 + quad(n) * 16;
      __builtin_amdgcn_raw_buffer_store_b128(o, rws, off, 0, SC1);
      __builtin_amdgcn_raw_buffer_store_b128(o, rws, off, 4 * A_STR, SC1);
      if (!hb && k < 2) {
        const int c0 = (x * 2 + k) * 32, col = c0 + (lane & 7) * 4 + g;
        const rsrc_t rout = mk(pout, 1024), rx = mk(px, 1024);
        const u32 xv = __builtin_amdgcn_raw_buffer_load_b16(rx, col * 2, 0, 0);
        bar();
        wait_until(t0, T_M);
        u32x4 m[1], v[1];
        gather<1>(m, rws, WS_M + x * R_STR + (g >> 1) * 8192 + ((lane >> 3) & 1) * 6144 + c0 * 4 + (lane & 7) * 16, {0}, T);
        float ga = rb(rs4(asf4(m[0])) * 0.5f), gb = dpp<ROR8>(ga);
        asm volatile("" : "+v"(ga), "+v"(gb));
        bar();
        gather<1>(v, rws, WS_D + x * R_STR + (lane >> 3) * 2048 + c0 * 4 + (lane & 7) * 16, {0}, T);
        float at = rs4(asf4(v[0]));
        at += dpp<ROR8>(at);
        float x1 = rb(blo(xv) + rb(ga * rb(at)));
        asm volatile("" : "+v"(x1));
        u32x4 e[7];
        gather2<7, 10>(e, rws, WS_E + (lane >> 3) * 2048 + c0 * 4 + (lane & 7) * 16, {0, 16384, 2 * 16384, 3 * 16384, 4 * 16384, 5 * 16384, 6 * 16384}, T);
        float d = rs4(((asf4(e[0]) + asf4(e[1])) + (asf4(e[2]) + asf4(e[3]))) + ((asf4(e[4]) + asf4(e[5])) + asf4(e[6])));
        d += dpp<ROR8>(d);
        const float r = x1 + rb(gb * rb(d));
        if ((lane & 8) == 0) __builtin_amdgcn_raw_buffer_store_b16((u16)pk(r, r), rout, col * 2, 0, 0);
        if (wg == 128 && lane == 0) __builtin_amdgcn_raw_buffer_store_b32(ev + 1u, rws, WS_EPOCH, 0, SC1);
      } else if (hb && (k < 6 || x < 4)) {
      bar();
      bar();
      wait_until(t0, T_M);
      u32x4 v[12];
      gather<12>(v, rws, WS_M + x * R_STR + lane * 16, {0, 1024, 2048, 3072, 4096, 5120, 8192, 9216, 10240, 11264, 12288, 13312}, T);
#pragma unroll
      for (int q = 0; q < 6; ++q) {
        const f32x4 a = asf4(v[q]) + asf4(v[q + 6]);
        f32x2 b = rb2(f32x2{a[0], a[1]}), c = rb2(f32x2{a[2], a[3]});
        if (q >= 4) { b = rb2(b + 1.f); c = rb2(c + 1.f); }
        *(f32x4*)(ms + q * 256 + lane * 4) = f32x4{b[0], b[1], c[0], c[1]};
      }
      bar();
      }
    }
  }
}

extern "C" void kernel_launch(void* const* d_in, const int* in_sizes, int n_in, void* d_out, int out_size, void* d_ws, size_t ws_size,
                              hipStream_t stream) {
  const char* const* p = (const char* const*)d_in;
  fused<<<256, 320, 0, stream>>>(p[0], p[1], p[2], p[3], p[9], p[10], (char*)d_ws, p[4], p[6], p[7], p[8], (char*)d_out);
}
```
